```python
import torch

def _softmax(x, axis=-1):
    return torch.softmax(x, dim=axis)
def _gelu(x):
    return torch.nn.functional.gelu(x, approximate="tanh")
def _silu(x):
    return torch.nn.functional.silu(x)
def _sigmoid(x):
    return torch.sigmoid(x)
def _layernorm(x, axis=-1, eps=1e-5):
    m = x.mean(dim=axis, keepdim=True)
    v = x.var(dim=axis, keepdim=True, unbiased=False)
    return (x - m) / torch.sqrt(v + eps)
def _scatter_add(tgt, idx, src, axis=0):
    return tgt.clone().index_add_(axis, idx, src)
def _segment_sum(src, seg, n):
    out = torch.zeros((n,) + src.shape[1:], dtype=src.dtype)
    return out.index_add_(0, seg, src)
def _pad(x, pw):
    flat = tuple(v for pair in reversed(pw) for v in pair)
    return torch.nn.functional.pad(x, flat)

kernel_name = 'parallel_sum__softmax_full_mha__moe_arch_mod__prompt'

def setup_inputs():

    _w = (lambda a: (lambda t: t.to(torch.bfloat16) if t.is_floating_point() else t)(torch.as_tensor(a)))
    def _A_setup():
        B, S, D = 1, 2048, 640
        return {
            "x": _w((lambda *s: torch.randn(*s, dtype=torch.bfloat16) * 0.05)(B, S, D)),
            "wqkv": _w((lambda *s: torch.randn(*s, dtype=torch.bfloat16) * 0.2)(D, 3 * D)),
            "wo": _w((lambda *s: torch.randn(*s, dtype=torch.bfloat16) * 0.2)(D, D)),
        }
    _A_in = _A_setup()

    def _B_setup():
        B, S, D, F, C = 1, 2048, 640, 1728, 2
        return {
            "x": _w((lambda *s: torch.randn(*s, dtype=torch.bfloat16) * 0.05)(B, S, D)),
            "w_r": _w((lambda *s: torch.randn(*s, dtype=torch.bfloat16) * 0.8)(D)),
            "w_gate": _w((lambda *s: torch.randn(*s, dtype=torch.bfloat16) * 0.2)(D, F)),
            "w_up": _w((lambda *s: torch.randn(*s, dtype=torch.bfloat16) * 0.2)(D, F)),
            "w_down": _w((lambda *s: torch.randn(*s, dtype=torch.bfloat16) * 0.2)(F, D)),
        }
    _B_in = _B_setup()

    return {
        "A_x": _A_in["x"],
        "A_wqkv": _A_in["wqkv"],
        "A_wo": _A_in["wo"],
        "B_x": _B_in["x"],
        "B_w_r": _B_in["w_r"],
        "B_w_gate": _B_in["w_gate"],
        "B_w_up": _B_in["w_up"],
        "B_w_down": _B_in["w_down"],
    }

def reference(A_x, A_wqkv, A_wo, B_x, B_w_r, B_w_gate, B_w_up, B_w_down):
    def _A_ref(x, wqkv, wo):
        B, S, D = x.shape
        H = 32; Dh = D // H
        qkv = (lambda x, *d: x.permute(*d))((x @ wqkv).reshape(B, S, 3, H, Dh), 2, 0, 3, 1, 4)
        q, k, v = qkv[0], qkv[1], qkv[2]
        scale = Dh ** -0.5
        scores = torch.einsum("bhqd,bhkd->bhqk", q, k) * scale
        p = _softmax(scores, axis=-1)
        o = torch.einsum("bhqk,bhkd->bhqd", p, v)
        return (lambda x, *d: x.permute(*d))(o, 0, 2, 1, 3).reshape(B, S, D) @ wo

    def _B_ref(x, w_r, w_gate, w_up, w_down):
        B, S, D = x.shape
        C = 2
        r = x @ w_r
        idx = torch.argsort(-r, axis=-1)[:, :C]
        idx_e = (lambda x, n, axis: torch.repeat_interleave(x, n, dim=axis))(idx.reshape(B, C, 1), D, axis=2)
        x_sel = (lambda x, i, axis: torch.take_along_dim(x, i, dim=axis))(x, idx_e, axis=1)
        g = _silu(x_sel @ w_gate)
        u = x_sel @ w_up
        y = (g * u) @ w_down
        r_sel = _sigmoid((lambda x, i, axis: torch.take_along_dim(x, i, dim=axis))(r, idx, axis=1))
        y = y * r_sel.reshape(B, C, 1)
        b_off = (torch.arange(B) * S).reshape(B, 1)
        flat_idx = (idx + b_off).reshape(B * C)
        zero = (x * 0.0).reshape(B * S, D)
        upd = _scatter_add(zero, flat_idx, y.reshape(B * C, D), axis=0)
        return x + upd.reshape(B, S, D)

    x = A_x
    xn = _layernorm(x, axis=-1)
    return x + _A_ref(xn, A_wqkv, A_wo) + _B_ref(xn, B_w_r, B_w_gate, B_w_up, B_w_down)
```

```cpp
#include <hip/hip_runtime.h>
#include <hip/hip_bf16.h>
#include <stdint.h>

typedef unsigned short u16;
typedef __attribute__((ext_vector_type(8))) __bf16 bf16x8;
typedef __attribute__((ext_vector_type(4))) __bf16 bf16x4;
typedef __attribute__((ext_vector_type(2))) __bf16 bf16x2;
typedef __attribute__((ext_vector_type(4))) short s16x4;
typedef __attribute__((ext_vector_type(16))) float f32x16;
typedef __attribute__((ext_vector_type(4))) float f32x4;
typedef __attribute__((ext_vector_type(4))) unsigned u32x4;
typedef __attribute__((ext_vector_type(2))) unsigned u32x2;
#define LDSP(T) __attribute__((address_space(3))) T*

constexpr int S_ = 2048, D_ = 640, NQKV = 1920, H_ = 32, DH = 20, F_ = 1728;
constexpr int KVT = 3072;
constexpr int NMOE = 108;
constexpr float QSCALE = 0.22360679774997896f * 1.4426950408889634f;

constexpr size_t WS_Q = 0;
constexpr size_t WS_KV = WS_Q + (size_t)H_ * S_ * DH * 2;
constexpr size_t WS_O = WS_KV + (size_t)H_ * 64 * KVT;
constexpr size_t WS_MEAN = WS_O + (size_t)S_ * D_ * 2;
constexpr size_t WS_RSTD = WS_MEAN + S_ * 4;
constexpr size_t WS_CAND = WS_RSTD + S_ * 4;
constexpr size_t WS_SEL = WS_CAND + 256;
constexpr size_t WS_YP = WS_SEL + 256;

__device__ __forceinline__ void glds16(const void* gsrc, unsigned lds_dst) {
  unsigned keep;
  asm volatile("s_mov_b32 %0, m0\n\ts_mov_b32 m0, %2\n\ts_nop 0\n\tglobal_load_lds_dwordx4 %1, off\n\ts_mov_b32 m0, %0"
               : "=&s"(keep) : "v"(gsrc), "s"(lds_dst) : "memory");
}
#define WAIT_VM(n) asm volatile("s_waitcnt vmcnt(" #n ")" ::: "memory")
__device__ __forceinline__ void bar() {
  asm volatile("s_waitcnt lgkmcnt(0)" ::: "memory");
  __builtin_amdgcn_s_barrier();
  asm volatile("" ::: "memory");
}
__device__ __forceinline__ unsigned pack2(float a, float b) {
  bf16x2 p; p[0] = (__bf16)a; p[1] = (__bf16)b; return __builtin_bit_cast(unsigned, p);
}
__device__ __forceinline__ float bflo(unsigned u) { return __builtin_bit_cast(float, u << 16); }
__device__ __forceinline__ float bfhi(unsigned u) { return __builtin_bit_cast(float, u & 0xffff0000u); }
__device__ __forceinline__ float dot2(unsigned a, unsigned b, float c) {
  return __builtin_amdgcn_fdot2_f32_bf16(__builtin_bit_cast(bf16x2, a), __builtin_bit_cast(bf16x2, b), c, false);
}
__device__ __forceinline__ f32x16 mfma32(u32x4 a, u32x4 b, f32x16 c) {
  return __builtin_amdgcn_mfma_f32_32x32x16_bf16(__builtin_bit_cast(bf16x8, a), __builtin_bit_cast(bf16x8, b), c, 0, 0, 0);
}
__device__ __forceinline__ f32x4 mfma16_32(u32x4 a, u32x4 b, f32x4 c) {
  return __builtin_amdgcn_mfma_f32_16x16x32_bf16(__builtin_bit_cast(bf16x8, a), __builtin_bit_cast(bf16x8, b), c, 0, 0, 0);
}
__device__ __forceinline__ f32x4 mfma16_16(u32x2 a, u32x2 b, f32x4 c) {
  return __builtin_amdgcn_mfma_f32_16x16x16bf16_1k(__builtin_bit_cast(s16x4, a), __builtin_bit_cast(s16x4, b), c, 0, 0, 0);
}
__device__ __forceinline__ u32x2 tr_read(unsigned lds_addr) {
  return __builtin_bit_cast(u32x2, __builtin_amdgcn_ds_read_tr16_b64_v4i16((LDSP(s16x4))(size_t)lds_addr));
}
__device__ __forceinline__ u32x4 lds128(unsigned a) { return *(LDSP(u32x4))(size_t)a; }
__device__ __forceinline__ unsigned lds32(unsigned a) { return *(LDSP(unsigned))(size_t)a; }
__device__ __forceinline__ unsigned ldsbase(void* p) { return (unsigned)(size_t)(LDSP(char))p; }

template <bool ISV>
__device__ __forceinline__ void k1_gemm(const u16* __restrict__ x, const u16* __restrict__ w, char* __restrict__ ws,
                                        char* smem, int mblk, int nblk) {
  const int tid = threadIdx.x, l = tid & 63, wv = __builtin_amdgcn_readfirstlane(tid >> 6);
  const int wm = wv >> 1, wn = wv & 1, r32 = l & 31, h = l >> 5;
  const unsigned sb = ldsbase(smem);
  const int m0 = mblk * 128, n0 = nblk * 128;
  const u16* asrc[4]; const u16* bsrc[4];
#pragma unroll
  for (int i = 0; i < 4; ++i) {
    int p = wv * 4 + i;
    int ra = p * 8 + (l >> 3), ca = (l & 7) ^ ((ra >> 1) & 7);
    asrc[i] = x + (size_t)(m0 + ra) * D_ + ca * 8;
    int rb = p * 4 + (l >> 4), cb = (l & 15) ^ (((rb & 3) << 2) | ((rb >> 2) & 3));
    bsrc[i] = w + (size_t)rb * NQKV + n0 + cb * 8;
  }
  auto issue = [&](int c) {
    unsigned st = sb + (c & 3) * 32768 + wv * 4096;
#pragma unroll
    for (int i = 0; i < 4; ++i) glds16(asrc[i] + c * 64, st + i * 1024);
#pragma unroll
    for (int i = 0; i < 4; ++i) glds16(bsrc[i] + (size_t)c * 64 * NQKV, st + 16384 + i * 1024);
  };
  issue(0); issue(1); issue(2);
  unsigned aoff[4], boff[2][2];
  {
    int row = wm * 64 + r32;
#pragma unroll
    for (int ks = 0; ks < 4; ++ks) aoff[ks] = row * 128 + (((ks * 2 + h) ^ ((row >> 1) & 7)) << 4);
    int g = l >> 4, q = (l >> 2) & 3, p = l & 3;
#pragma unroll
    for (int t = 0; t < 2; ++t)
#pragma unroll
      for (int nt = 0; nt < 2; ++nt) {
        int rr = 8 * (g >> 1) + 4 * t + q;
        int ch = (wn * 64 + nt * 32 + 16 * (g & 1)) / 8 + (p >> 1);
        boff[t][nt] = 16384 + rr * 256 + ((ch ^ (((rr & 3) << 2) | ((rr >> 2) & 3))) << 4) + 8 * (p & 1);
      }
  }
  f32x16 acc[2][2] = {};
  float sx[2] = {0, 0}, sq[2] = {0, 0}, cs[2] = {0, 0};
  for (int c = 0; c < 10; ++c) {
    if (c < 8) WAIT_VM(16); else if (c == 8) WAIT_VM(8); else WAIT_VM(0);
    bar();
    if (c + 3 < 10) issue(c + 3);
    unsigned st = sb + (c & 3) * 32768;
#pragma unroll
    for (int ks = 0; ks < 4; ++ks) {
      u32x4 xa[2], wb[2];
#pragma unroll
      for (int mt = 0; mt < 2; ++mt) xa[mt] = lds128(st + aoff[ks] + mt * 4096);
#pragma unroll
      for (int nt = 0; nt < 2; ++nt) {
        u32x2 lo = tr_read(st + boff[0][nt] + ks * 4096), hi = tr_read(st + boff[1][nt] + ks * 4096);
        wb[nt] = u32x4{lo[0], lo[1], hi[0], hi[1]};
      }
#pragma unroll
      for (int mt = 0; mt < 2; ++mt)
#pragma unroll
        for (int nt = 0; nt < 2; ++nt)
          acc[mt][nt] = ISV ? mfma32(xa[mt], wb[nt], acc[mt][nt]) : mfma32(wb[nt], xa[mt], acc[mt][nt]);
#pragma unroll
      for (int i = 0; i < 2; ++i)
#pragma unroll
        for (int j = 0; j < 4; ++j) {
          sx[i] = dot2(xa[i][j], 0x3f803f80u, sx[i]);
          sq[i] = dot2(xa[i][j], xa[i][j], sq[i]);
          cs[i] = dot2(wb[i][j], 0x3f803f80u, cs[i]);
        }
    }
  }
  bar();
  float mean[2], rstd[2];
  LDSP(float) st = (LDSP(float))(size_t)(sb + wv * 1024);
#pragma unroll
  for (int i = 0; i < 2; ++i) {
    float a = sx[i] + __shfl_xor(sx[i], 32), b = sq[i] + __shfl_xor(sq[i], 32);
    cs[i] += __shfl_xor(cs[i], 32);
    mean[i] = a * (1.f / D_);
    float var = fmaxf(b * (1.f / D_) - mean[i] * mean[i], 0.f);
    rstd[i] = 1.f / sqrtf(var + 1e-5f);
    if (h == 0) {
      if (ISV) { st[i * 32 + r32] = mean[i]; st[64 + i * 32 + r32] = rstd[i]; }
      else st[i * 32 + r32] = cs[i];
    }
  }
  asm volatile("s_waitcnt lgkmcnt(0)" ::: "memory");
  __builtin_amdgcn_wave_barrier();
  if (!ISV) {
    const bool isq = nblk < 5;
    const float sc = isq ? QSCALE : 1.f;
#pragma unroll
    for (int mt = 0; mt < 2; ++mt) {
      const int tok = m0 + wm * 64 + mt * 32 + r32;
      const float rs = rstd[mt] * sc, mu = mean[mt];
#pragma unroll
      for (int nt = 0; nt < 2; ++nt)
#pragma unroll
        for (int g = 0; g < 4; ++g) {
          int nl = nt * 32 + 8 * g + 4 * h;
          f32x4 c4 = *(LDSP(f32x4))(st + nl);
          float v0 = (acc[mt][nt][4 * g] - mu * c4[0]) * rs, v1 = (acc[mt][nt][4 * g + 1] - mu * c4[1]) * rs;
          float v2 = (acc[mt][nt][4 * g + 2] - mu * c4[2]) * rs, v3 = (acc[mt][nt][4 * g + 3] - mu * c4[3]) * rs;
          unsigned p0 = pack2(v0, v1), p1 = pack2(v2, v3);
          int n = (n0 + wn * 64 + nl) % D_, hd = n / DH, d = n - hd * DH;
          if (isq) {
            *(u32x2*)(ws + WS_Q + ((size_t)(hd * S_ + tok) * DH + d) * 2) = u32x2{p0, p1};
          } else {
            char* img = ws + WS_KV + (size_t)(hd * 64 + (tok >> 5)) * KVT;
            if (d < 16) *(u32x2*)(img + (d >> 3) * 512 + (tok & 31) * 16 + (d & 7) * 2) = u32x2{p0, p1};
            else { *(unsigned*)(img + 1024 + (tok & 31) * 4) = p0; *(unsigned*)(img + 1152 + (tok & 31) * 4) = p1; }
          }
        }
    }
  } else {
#pragma unroll
    for (int nt = 0; nt < 2; ++nt) {
      const int n = (n0 + wn * 64 + nt * 32 + r32) - 2 * D_, hd = n / DH, d = n - hd * DH;
#pragma unroll
      for (int mt = 0; mt < 2; ++mt) {
        char* img = ws + WS_KV + (size_t)(hd * 64 + ((m0 + wm * 64 + mt * 32) >> 5)) * KVT + 1280;
#pragma unroll
        for (int s = 0; s < 2; ++s) {
          f32x4 ma = *(LDSP(f32x4))(st + mt * 32 + 16 * s + 4 * h), mb = *(LDSP(f32x4))(st + mt * 32 + 16 * s + 8 + 4 * h);
          f32x4 ra = *(LDSP(f32x4))(st + 64 + mt * 32 + 16 * s + 4 * h), rb = *(LDSP(f32x4))(st + 64 + mt * 32 + 16 * s + 8 + 4 * h);
          float v[8];
#pragma unroll
          for (int j = 0; j < 4; ++j) {
            v[j] = (acc[mt][nt][8 * s + j] - ma[j] * cs[nt]) * ra[j];
            v[4 + j] = (acc[mt][nt][8 * s + 4 + j] - mb[j] * cs[nt]) * rb[j];
          }
          char* dst = img + (s * 2 + h) * 384 + d * 16;
          *(u32x4*)dst = u32x4{pack2(v[0], v[1]), pack2(v[2], v[3]), pack2(v[4], v[5]), pack2(v[6], v[7])};
          if (d == DH - 1) *(u32x4*)(dst + 16) = u32x4{0x3f803f80u, 0x3f803f80u, 0x3f803f80u, 0x3f803f80u};
        }
      }
    }
  }
}

__device__ __forceinline__ void k1_stats(const u16* __restrict__ x, const u16* __restrict__ wr, char* __restrict__ ws,
                                         char* smem, int sb) {
  const int tid = threadIdx.x, l = tid & 63, wv = tid >> 6, j = l & 15, sub = l >> 4;
  u32x4 wv4[5];
#pragma unroll
  for (int i = 0; i < 5; ++i) wv4[i] = *(const u32x4*)(wr + (i * 16 + j) * 8);
  float* rs = (float*)smem;
#pragma unroll 2
  for (int it = 0; it < 8; ++it) {
    const int tl = wv * 32 + it * 4 + sub, tok = sb * 128 + tl;
    u32x4 xv[5];
#pragma unroll
    for (int i = 0; i < 5; ++i) xv[i] = *(const u32x4*)(x + (size_t)tok * D_ + (i * 16 + j) * 8);
    float s = 0;
#pragma unroll
    for (int i = 0; i < 5; ++i)
#pragma unroll
      for (int k = 0; k < 4; ++k) s += bflo(xv[i][k]) + bfhi(xv[i][k]);
#pragma unroll
    for (int o = 8; o; o >>= 1) s += __shfl_xor(s, o);
    const float mu = s * (1.f / D_);
    float v = 0;
#pragma unroll
    for (int i = 0; i < 5; ++i)
#pragma unroll
      for (int k = 0; k < 4; ++k) { float a = bflo(xv[i][k]) - mu, b = bfhi(xv[i][k]) - mu; v += a * a + b * b; }
#pragma unroll
    for (int o = 8; o; o >>= 1) v += __shfl_xor(v, o);
    const float rstd = 1.f / sqrtf(v * (1.f / D_) + 1e-5f);
    float r = 0;
#pragma unroll
    for (int i = 0; i < 5; ++i)
#pragma unroll
      for (int k = 0; k < 4; ++k) {
        unsigned p = pack2((bflo(xv[i][k]) - mu) * rstd, (bfhi(xv[i][k]) - mu) * rstd);
        r += bflo(p) * bflo(wv4[i][k]) + bfhi(p) * bfhi(wv4[i][k]);
      }
#pragma unroll
    for (int o = 8; o; o >>= 1) r += __shfl_xor(r, o);
    if (j == 0) {
      ((float*)(ws + WS_MEAN))[tok] = mu; ((float*)(ws + WS_RSTD))[tok] = rstd; rs[tl] = r;
    }
  }
  __syncthreads();
  if (wv == 0) {
    float a = rs[l], b = rs[l + 64]; int ia = l, ib = l + 64;
    if (b > a) { float t = a; a = b; b = t; int u = ia; ia = ib; ib = u; }
#pragma unroll
    for (int o = 32; o; o >>= 1) {
      float oa = __shfl_xor(a, o), ob = __shfl_xor(b, o); int oia = __shfl_xor(ia, o), oib = __shfl_xor(ib, o);
      bool w = oa > a || (oa == a && oia < ia);
      float na = w ? oa : a, nb1 = w ? a : oa, nb2 = w ? ob : b; int nia = w ? oia : ia, ni1 = w ? ia : oia, ni2 = w ? oib : ib;
      bool w2 = nb1 > nb2 || (nb1 == nb2 && ni1 < ni2);
      a = na; ia = nia; b = w2 ? nb1 : nb2; ib = w2 ? ni1 : ni2;
    }
    if (l == 0) {
      float* c = (float*)(ws + WS_CAND) + sb * 4;
      c[0] = a; ((int*)c)[1] = sb * 128 + ia; c[2] = b; ((int*)c)[3] = sb * 128 + ib;
    }
  }
}

__global__ __launch_bounds__(256, 1) void k1_qkv(const u16* __restrict__ x, const u16* __restrict__ w,
                                                 const u16* __restrict__ wr, char* __restrict__ ws) {
  __shared__ __attribute__((aligned(16))) char smem[4 * 32768];
  const int bid = blockIdx.x;
  if (bid >= 240) { k1_stats(x, wr, ws, smem, bid - 240); return; }
  const int mblk = (bid & 7) * 2 + (bid >> 3) / 15, nblk = (bid >> 3) % 15;
  if (nblk >= 10) k1_gemm<true>(x, w, ws, smem, mblk, nblk);
  else k1_gemm<false>(x, w, ws, smem, mblk, nblk);
}

constexpr int K2_RING = 0, K2_WGU = 73728, K2_WD = K2_WGU + 40960, K2_XS = K2_WD + 20480, K2_SEL = K2_XS + 2560,
              K2_LDS = K2_SEL + 64;
constexpr unsigned PTHR = (127u + 80u) << 7;

__device__ __forceinline__ unsigned pkmax3(unsigned a, unsigned b, unsigned c) {
  unsigned r; asm("v_pk_maximum3_f16 %0, %1, %2, %3" : "=v"(r) : "v"(a), "v"(b), "v"(c)); return r;
}
__device__ __forceinline__ void softmax_pack(const f32x16& s, u32x4& p0, u32x4& p1) {
#pragma unroll
  for (int i = 0; i < 4; ++i) {
    p0[i] = pack2(__builtin_amdgcn_exp2f(s[2 * i]), __builtin_amdgcn_exp2f(s[2 * i + 1]));
    p1[i] = pack2(__builtin_amdgcn_exp2f(s[8 + 2 * i]), __builtin_amdgcn_exp2f(s[9 + 2 * i]));
  }
}

__global__ __launch_bounds__(512, 2) void k2_attn(const u16* __restrict__ x, const u16* __restrict__ wg,
                                                  const u16* __restrict__ wu, const u16* __restrict__ wd,
                                                  char* __restrict__ ws) {
  __shared__ __attribute__((aligned(16))) char smem[K2_LDS];
  const int tid = threadIdx.x, l = tid & 63, wv = __builtin_amdgcn_readfirstlane(tid >> 6);
  const int qg = wv & 3, hf = wv >> 2, r32 = l & 31, h = l >> 5;
  const int bid = blockIdx.x, head = (bid & 7) * 4 + ((bid >> 3) & 3), qb = bid >> 5;
  const bool moe = bid < NMOE;
  const unsigned sb = ldsbase(smem);
  const char* kvsrc = ws + WS_KV + (size_t)(head * 64 + hf * 32) * KVT + qg * 3072 + l * 16;
  auto issue = [&](int c) {
    unsigned dst = sb + K2_RING + (c % 3) * 24576 + hf * 12288 + qg * 3072;
#pragma unroll
    for (int i = 0; i < 3; ++i) glds16(kvsrc + (size_t)c * 12288 + i * 1024, dst + i * 1024);
  };
  u32x4 q1[2], q2[2];
#pragma unroll
  for (int t = 0; t < 2; ++t) {
    const char* qp = ws + WS_Q + ((size_t)head * S_ + qb * 256 + qg * 64 + t * 32 + r32) * (DH * 2);
    u32x2 a = *(const u32x2*)(qp + h * 16), b = *(const u32x2*)(qp + h * 16 + 8);
    q1[t] = u32x4{a[0], a[1], b[0], b[1]};
    q2[t] = u32x4{*(const unsigned*)(qp + 32 + h * 4), 0u, 0u, 0u};
  }
  issue(0); issue(1);
  f32x16 o[2] = {};
  float m[2] = {0.f, 0.f};
  bool first = true;
  const unsigned koff = l * 16, k2off = 1024 + l * 4, voff = 1280 + h * 384 + r32 * 16;

  for (int c = 0; c < 8; ++c) {
    if (c == 7) WAIT_VM(0);
    else if (moe && (c == 1 || c == 2)) WAIT_VM(11);
    else WAIT_VM(3);
    bar();
    if (c + 2 < 8) issue(c + 2);
    if (moe) {
      if (c == 0) {
#pragma unroll
        for (int i = 0; i < 8; ++i) {
          int p = min(wv * 8 + i, 59);
          const u16* src;
          if (p < 40) src = ((l & 2) ? wu : wg) + (size_t)(p * 16 + (l >> 2)) * F_ + bid * 16 + (l & 1) * 8;
          else src = wd + (size_t)bid * 16 * D_ + (p - 40) * 512 + l * 8;
          glds16(src, sb + K2_WGU + p * 1024);
        }
      } else if (c == 1 && wv == 0) {
        const float* cd = (const float*)(ws + WS_CAND);
        float a = l < 32 ? cd[l * 2] : -INFINITY; int ia = l < 32 ? ((const int*)cd)[l * 2 + 1] : 0x7fffffff;
        float b = -INFINITY; int ib = 0x7fffffff;
#pragma unroll
        for (int of = 32; of; of >>= 1) {
          float oa = __shfl_xor(a, of), ob = __shfl_xor(b, of); int oia = __shfl_xor(ia, of), oib = __shfl_xor(ib, of);
          bool w = oa > a || (oa == a && oia < ia);
          float na = w ? oa : a, nb1 = w ? a : oa, nb2 = w ? ob : b; int nia = w ? oia : ia, ni1 = w ? ia : oia, ni2 = w ? oib : ib;
          bool w2 = nb1 > nb2 || (nb1 == nb2 && ni1 < ni2);
          a = na; ia = nia; b = w2 ? nb1 : nb2; ib = w2 ? ni1 : ni2;
        }
        if (l == 0) {
          LDSP(int) sp = (LDSP(int))(size_t)(sb + K2_SEL);
          sp[0] = ia; sp[1] = ib;
          if (bid == 0) {
            int* g = (int*)(ws + WS_SEL);
            g[0] = ia; g[1] = ib;
            ((float*)g)[2] = 1.f / (1.f + __expf(-a)); ((float*)g)[3] = 1.f / (1.f + __expf(-b));
          }
        }
      } else if (c == 2 && wv == 1) {
        LDSP(int) sp = (LDSP(int))(size_t)(sb + K2_SEL);
#pragma unroll
        for (int i = 0; i < 3; ++i) {
          int e = i * 64 + l;
          if (e < 160) {
            int tk = e / 80, ch = e - tk * 80, row = sp[tk];
            u32x4 v = *(const u32x4*)(x + (size_t)row * D_ + ch * 8);
            float mu = ((const float*)(ws + WS_MEAN))[row], rs = ((const float*)(ws + WS_RSTD))[row];
            u32x4 r;
#pragma unroll
            for (int k = 0; k < 4; ++k) r[k] = pack2((bflo(v[k]) - mu) * rs, (bfhi(v[k]) - mu) * rs);
            *(LDSP(u32x4))(size_t)(sb + K2_XS + tk * 1280 + ch * 16) = r;
          }
        }
      } else if (c == 3 && wv == 2) {
        const int g = l >> 4, q = (l >> 2) & 3, p = l & 3, col = l & 15;
        f32x4 ag = {0, 0, 0, 0}, au = {0, 0, 0, 0};
        const unsigned wa = sb + K2_WGU + (8 * g + q) * 64 + p * 8, xa = sb + K2_XS + (col & 1) * 1280 + g * 16;
#pragma unroll 4
        for (int s = 0; s < 20; ++s) {
          u32x4 xb = lds128(xa + s * 64);
          if (col >= 2) xb = u32x4{0, 0, 0, 0};
          u32x2 g0 = tr_read(wa + s * 2048), g1 = tr_read(wa + s * 2048 + 256);
          u32x2 u0 = tr_read(wa + s * 2048 + 32), u1 = tr_read(wa + s * 2048 + 256 + 32);
          ag = mfma16_32(u32x4{g0[0], g0[1], g1[0], g1[1]}, xb, ag);
          au = mfma16_32(u32x4{u0[0], u0[1], u1[0], u1[1]}, xb, au);
        }
        float hv[4];
#pragma unroll
        for (int i = 0; i < 4; ++i) hv[i] = ag[i] / (1.f + __expf(-ag[i])) * au[i];
        const u32x2 hb = {pack2(hv[0], hv[1]), pack2(hv[2], hv[3])};
        const unsigned da = sb + K2_WD + (4 * g + q) * 1280 + p * 8;
        float* yp = (float*)(ws + WS_YP) + ((size_t)bid * 2 + (col & 1)) * D_ + 4 * g;
#pragma unroll 4
        for (int t = 0; t < 40; ++t) {
          f32x4 y = mfma16_16(tr_read(da + t * 32), hb, f32x4{0, 0, 0, 0});
          if (col < 2) *(f32x4*)(yp + t * 16) = y;
        }
      }
    }
    const unsigned st = sb + K2_RING + (c % 3) * 24576 + hf * 12288;
    f32x16 s[2][2];
    u32x4 vf[2][2];
    auto qk = [&](int T, f32x16* d) {
      u32x4 k1 = lds128(st + T * KVT + koff);
      u32x4 k2 = {lds32(st + T * KVT + k2off), 0x3f803f80u, 0u, 0u};
      const f32x16 z = {};
#pragma unroll
      for (int t = 0; t < 2; ++t) d[t] = mfma32(k2, q2[t], mfma32(k1, q1[t], z));
    };
    qk(0, s[0]);
#pragma unroll
    for (int T = 0; T < 4; ++T) {
      f32x16* cur = s[T & 1];
      f32x16* nxt = s[(T + 1) & 1];
      vf[0][0] = lds128(st + T * KVT + voff);
      vf[0][1] = lds128(st + T * KVT + voff + 768);
      if (T < 3) qk(T + 1, nxt);
      u32x4 p[2][2];
      softmax_pack(cur[0], p[0][0], p[0][1]);
      softmax_pack(cur[1], p[1][0], p[1][1]);
      unsigned pm = pkmax3(p[0][0][0], p[0][0][1], p[0][0][2]);
      pm = pkmax3(pm, p[0][0][3], p[0][1][0]); pm = pkmax3(pm, p[0][1][1], p[0][1][2]);
      unsigned pn = pkmax3(p[0][1][3], p[1][0][0], p[1][0][1]);
      pn = pkmax3(pn, p[1][0][2], p[1][0][3]); pn = pkmax3(pn, p[1][1][0], p[1][1][1]);
      pm = pkmax3(pm, pn, p[1][1][2]); pm = pkmax3(pm, p[1][1][3], p[1][1][3]);
      const bool over = (pm & 0xffffu) > PTHR || (pm >> 16) > PTHR;
      if (__builtin_expect(first || __any(over), 0)) {
#pragma unroll
        for (int t = 0; t < 2; ++t) {
          float tm = fmaxf(cur[t][0], cur[t][1]);
#pragma unroll
          for (int i = 2; i < 16; ++i) tm = fmaxf(tm, cur[t][i]);
          tm = fmaxf(tm, __shfl_xor(tm, 32));
          if (!first) tm = fmaxf(tm, 0.f);
          const float mn = m[t] + tm, dl = mn - m[t];
          const float al = first ? 0.f : __builtin_amdgcn_exp2f(-dl);
          m[t] = mn;
#pragma unroll
          for (int i = 0; i < 12; ++i) o[t][i] *= al;
#pragma unroll
          for (int i = 0; i < 16; ++i) cur[t][i] -= dl;
          if (T < 3) {
#pragma unroll
            for (int i = 0; i < 16; ++i) nxt[t][i] -= dl;
          }
          const unsigned uh = __builtin_bit_cast(unsigned, -mn) & 0xffff0000u;
          const float r1 = -mn - __builtin_bit_cast(float, uh);
          const unsigned um = __builtin_bit_cast(unsigned, r1) & 0xffff0000u;
          const float r2 = r1 - __builtin_bit_cast(float, um);
          q2[t][1] = h == 0 ? ((uh >> 16) | um) : (__builtin_bit_cast(unsigned, r2) >> 16);
          softmax_pack(cur[t], p[t][0], p[t][1]);
        }
        first = false;
      }
#pragma unroll
      for (int t = 0; t < 2; ++t) {
        o[t] = mfma32(vf[0][0], p[t][0], o[t]);
        o[t] = mfma32(vf[0][1], p[t][1], o[t]);
      }
    }
  }
  bar();
  LDSP(float) mb = (LDSP(float))(size_t)(sb + qg * 6656);
  if (hf == 1) {
#pragma unroll
    for (int t = 0; t < 2; ++t) {
#pragma unroll
      for (int i = 0; i < 12; ++i) mb[(t * 13 + i) * 64 + l] = o[t][i];
      mb[(t * 13 + 12) * 64 + l] = m[t];
    }
  }
  bar();
  if (hf == 0) {
#pragma unroll
    for (int t = 0; t < 2; ++t) {
      const float mo = mb[(t * 13 + 12) * 64 + l], mm = fmaxf(mo, m[t]);
      const float a = __builtin_amdgcn_exp2f(m[t] - mm), b = __builtin_amdgcn_exp2f(mo - mm);
      float r[12];
#pragma unroll
      for (int i = 0; i < 12; ++i) r[i] = o[t][i] * a + mb[(t * 13 + i) * 64 + l] * b;
      const float inv = 1.f / __shfl(r[8], r32 + 32);
      char* dst = ws + WS_O + ((size_t)(qb * 256 + qg * 64 + t * 32 + r32) * D_ + head * DH) * 2;
      *(u32x2*)(dst + h * 8) = u32x2{pack2(r[0] * inv, r[1] * inv), pack2(r[2] * inv, r[3] * inv)};
      *(u32x2*)(dst + 16 + h * 8) = u32x2{pack2(r[4] * inv, r[5] * inv), pack2(r[6] * inv, r[7] * inv)};
      if (h == 0) *(u32x2*)(dst + 32) = u32x2{pack2(r[8] * inv, r[9] * inv), pack2(r[10] * inv, r[11] * inv)};
    }
  }
}

__global__ __launch_bounds__(256, 1) void k3_out(const u16* __restrict__ x, const u16* __restrict__ wo,
                                                 const char* __restrict__ ws, u16* __restrict__ out) {
  __shared__ __attribute__((aligned(16))) char smem[6 * 24576];
  const int tid = threadIdx.x, l = tid & 63, wv = __builtin_amdgcn_readfirstlane(tid >> 6);
  const int r32 = l & 31, h = l >> 5, bid = blockIdx.x;
  const int mblk = (bid & 7) * 8 + ((bid >> 3) & 7), nblk = bid >> 6;
  const int m0 = mblk * 32, n0 = nblk * 160;
  const unsigned sb = ldsbase(smem);
  const u16* og = (const u16*)(ws + WS_O);
  const u16* src[6];
#pragma unroll
  for (int i = 0; i < 6; ++i) {
    int p = wv * 6 + i;
    if (p < 4) { int ra = p * 8 + (l >> 3), ca = (l & 7) ^ ((ra >> 1) & 7); src[i] = og + (size_t)(m0 + ra) * D_ + ca * 8; }
    else { int e = (p - 4) * 64 + l, rb = e / 20, cb = e - rb * 20; src[i] = wo + (size_t)rb * D_ + n0 + cb * 8; }
  }
  auto issue = [&](int c) {
#pragma unroll
    for (int i = 0; i < 6; ++i) {
      int p = wv * 6 + i;
      glds16(src[i] + (p < 4 ? (size_t)c * 64 : (size_t)c * 64 * D_), sb + (c % 6) * 24576 + p * 1024);
    }
  };
#pragma unroll
  for (int c = 0; c < 5; ++c) issue(c);
  const int er = tid >> 3, ec = (tid & 7) * 20, tok = m0 + er;
  const int* sel = (const int*)(ws + WS_SEL);
  const int i0 = sel[0], i1 = sel[1];
  const float mu = ((const float*)(ws + WS_MEAN))[tok], rs = ((const float*)(ws + WS_RSTD))[tok];
  u32x2 xv[5];
#pragma unroll
  for (int i = 0; i < 5; ++i) xv[i] = *(const u32x2*)(x + (size_t)tok * D_ + n0 + ec + i * 4);

  const unsigned aoff = r32 * 128 + (((wv * 2 + h) ^ ((r32 >> 1) & 7)) << 4);
  const int g = l >> 4, q = (l >> 2) & 3, p4 = l & 3;
  const unsigned boff = 4096 + (wv * 16 + 8 * (g >> 1) + q) * 320 + (16 * (g & 1) + 4 * p4) * 2;
  f32x16 acc[5] = {};
  for (int c = 0; c < 10; ++c) {
    if (c <= 5) WAIT_VM(24); else if (c == 6) WAIT_VM(18); else if (c == 7) WAIT_VM(12); else if (c == 8) WAIT_VM(6); else WAIT_VM(0);
    bar();
    if (c + 5 < 10) issue(c + 5);
    const unsigned st = sb + (c % 6) * 24576;
    u32x4 a = lds128(st + aoff);
#pragma unroll
    for (int nt = 0; nt < 5; ++nt) {
      u32x2 lo = tr_read(st + boff + nt * 64), hi = tr_read(st + boff + nt * 64 + 4 * 320);
      acc[nt] = mfma32(a, u32x4{lo[0], lo[1], hi[0], hi[1]}, acc[nt]);
    }
  }
  bar();
  LDSP(float) pr = (LDSP(float))(size_t)(sb);
#pragma unroll
  for (int nt = 0; nt < 5; ++nt)
#pragma unroll
    for (int r = 0; r < 16; ++r) {
      int row = (r & 3) + 8 * (r >> 2) + 4 * h;
      pr[(wv * 32 + row) * 164 + nt * 32 + r32] = acc[nt][r];
    }
  bar();
  float upd[20];
#pragma unroll
  for (int i = 0; i < 20; ++i) upd[i] = 0.f;
  if (tok == i0 || tok == i1) {
    const int tk = tok == i0 ? 0 : 1;
    const float gate = ((const float*)sel)[2 + tk];
    const float* yp = (const float*)(ws + WS_YP) + tk * D_ + n0 + ec;
    for (int w = 0; w < NMOE; ++w) {
#pragma unroll
      for (int i = 0; i < 5; ++i) {
        f32x4 v = *(const f32x4*)(yp + (size_t)w * 2 * D_ + i * 4);
#pragma unroll
        for (int j = 0; j < 4; ++j) upd[i * 4 + j] += v[j];
      }
    }
#pragma unroll
    for (int i = 0; i < 20; ++i) upd[i] *= gate;
  }
#pragma unroll
  for (int i = 0; i < 5; ++i) {
    f32x4 v = *(LDSP(f32x4))(pr + er * 164 + ec + i * 4);
#pragma unroll
    for (int w = 1; w < 4; ++w) v += *(LDSP(f32x4))(pr + (w * 32 + er) * 164 + ec + i * 4);
    float xs[4] = {bflo(xv[i][0]), bfhi(xv[i][0]), bflo(xv[i][1]), bfhi(xv[i][1])};
    float ov[4];
#pragma unroll
    for (int j = 0; j < 4; ++j) ov[j] = xs[j] + (xs[j] - mu) * rs + v[j] + upd[i * 4 + j];
    *(u32x2*)(out + (size_t)tok * D_ + n0 + ec + i * 4) = u32x2{pack2(ov[0], ov[1]), pack2(ov[2], ov[3])};
  }
}

extern "C" void kernel_launch(void* const* d_in, const int* in_sizes, int n_in, void* d_out, int out_size, void* d_ws,
                              size_t ws_size, hipStream_t stream) {
  const u16* x = (const u16*)d_in[0];
  const u16* wqkv = (const u16*)d_in[1];
  const u16* wo = (const u16*)d_in[2];
  const u16* wr = (const u16*)d_in[4];
  const u16* wg = (const u16*)d_in[5];
  const u16* wu = (const u16*)d_in[6];
  const u16* wd = (const u16*)d_in[7];
  char* ws = (char*)d_ws;
  k1_qkv<<<256, 256, 0, stream>>>(x, wqkv, wr, ws);
  k2_attn<<<256, 512, 0, stream>>>(x, wg, wu, wd, ws);
  k3_out<<<256, 256, 0, stream>>>(x, wo, ws, (u16*)d_out);
}
```

```cpp
#include <hip/hip_runtime.h>
#include <hip/hip_bf16.h>
#include <stdint.h>

typedef unsigned short u16;
typedef __attribute__((ext_vector_type(8))) __bf16 bf16x8;
typedef __attribute__((ext_vector_type(4))) __bf16 bf16x4;
typedef __attribute__((ext_vector_type(2))) __bf16 bf16x2;
typedef __attribute__((ext_vector_type(4))) short s16x4;
typedef __attribute__((ext_vector_type(16))) float f32x16;
typedef __attribute__((ext_vector_type(4))) float f32x4;
typedef __attribute__((ext_vector_type(2))) float f32x2;
typedef __attribute__((ext_vector_type(4))) unsigned u32x4;
typedef __attribute__((ext_vector_type(2))) unsigned u32x2;
#define LDSP(T) __attribute__((address_space(3))) T*

constexpr int S_ = 2048, D_ = 640, NQKV = 1920, H_ = 32, DH = 20, F_ = 1728;
constexpr int KVT = 3072;
constexpr int NMOE = 108;
constexpr float QSCALE = 0.22360679774997896f * 1.4426950408889634f;

constexpr size_t WS_QK = 0;
constexpr size_t WS_V = WS_QK + (size_t)64 * 160 * 512;
constexpr size_t WS_O = WS_V + (size_t)H_ * 64 * 1536;
constexpr size_t WS_MEAN = WS_O + (size_t)S_ * D_ * 2;
constexpr size_t WS_RSTD = WS_MEAN + S_ * 4;
constexpr size_t WS_CAND = WS_RSTD + S_ * 4;
constexpr size_t WS_SEL = WS_CAND + 256;
constexpr size_t WS_YP = WS_SEL + 256;

__device__ __forceinline__ void glds16(const void* gsrc, unsigned lds_dst) {
  unsigned keep;
  asm volatile("s_mov_b32 %0, m0\n\ts_mov_b32 m0, %2\n\ts_nop 0\n\tglobal_load_lds_dwordx4 %1, off\n\ts_mov_b32 m0, %0"
               : "=&s"(keep) : "v"(gsrc), "s"(lds_dst) : "memory");
}
__device__ __forceinline__ void glds4(const void* gsrc, unsigned lds_dst) {
  unsigned keep;
  asm volatile("s_mov_b32 %0, m0\n\ts_mov_b32 m0, %2\n\ts_nop 0\n\tglobal_load_lds_dword %1, off\n\ts_mov_b32 m0, %0"
               : "=&s"(keep) : "v"(gsrc), "s"(lds_dst) : "memory");
}
__device__ __forceinline__ void set_m0(unsigned lds_dst) { asm volatile("s_mov_b32 m0, %0\n\ts_nop 0" :: "s"(lds_dst) : "memory"); }
template <int OFF> __device__ __forceinline__ void glds_s(unsigned voff, const void* sbase) {
  asm volatile("global_load_lds_dwordx4 %0, %1 offset:%2" :: "v"(voff), "s"(sbase), "n"(OFF) : "memory");
}
#define WAIT_VM(n) asm volatile("s_waitcnt vmcnt(" #n ")" ::: "memory")
__device__ __forceinline__ void bar() {
  asm volatile("s_waitcnt lgkmcnt(0)" ::: "memory");
  __builtin_amdgcn_s_barrier();
  asm volatile("" ::: "memory");
}
__device__ __forceinline__ unsigned pack2(float a, float b) {
  bf16x2 p; p[0] = (__bf16)a; p[1] = (__bf16)b; return __builtin_bit_cast(unsigned, p);
}
__device__ __forceinline__ float bflo(unsigned u) { return __builtin_bit_cast(float, u << 16); }
__device__ __forceinline__ float bfhi(unsigned u) { return __builtin_bit_cast(float, u & 0xffff0000u); }
__device__ __forceinline__ float dot2(unsigned a, unsigned b, float c) {
  return __builtin_amdgcn_fdot2_f32_bf16(__builtin_bit_cast(bf16x2, a), __builtin_bit_cast(bf16x2, b), c, false);
}
__device__ __forceinline__ f32x16 mfma32(u32x4 a, u32x4 b, f32x16 c) {
  return __builtin_amdgcn_mfma_f32_32x32x16_bf16(__builtin_bit_cast(bf16x8, a), __builtin_bit_cast(bf16x8, b), c, 0, 0, 0);
}
__device__ __forceinline__ f32x4 mfma16_32(u32x4 a, u32x4 b, f32x4 c) {
  return __builtin_amdgcn_mfma_f32_16x16x32_bf16(__builtin_bit_cast(bf16x8, a), __builtin_bit_cast(bf16x8, b), c, 0, 0, 0);
}
__device__ __forceinline__ f32x4 mfma16_16(u32x2 a, u32x2 b, f32x4 c) {
  return __builtin_amdgcn_mfma_f32_16x16x16bf16_1k(__builtin_bit_cast(s16x4, a), __builtin_bit_cast(s16x4, b), c, 0, 0, 0);
}
__device__ __forceinline__ u32x2 tr_read(unsigned lds_addr) {
  return __builtin_bit_cast(u32x2, __builtin_amdgcn_ds_read_tr16_b64_v4i16((LDSP(s16x4))(size_t)lds_addr));
}
__device__ __forceinline__ u32x4 lds128(unsigned a) { return *(LDSP(u32x4))(size_t)a; }
__device__ __forceinline__ unsigned lds32(unsigned a) { return *(LDSP(unsigned))(size_t)a; }
__device__ __forceinline__ unsigned ldsbase(void* p) { return (unsigned)(size_t)(LDSP(char))p; }

#define ST(i)
#define SCHED_FENCE() __builtin_amdgcn_sched_barrier(0)
#define SGB(m, n) __builtin_amdgcn_sched_group_barrier(m, n, 0)
template <bool ISV, bool RT>
__device__ __forceinline__ void k1_gemm(const u16* __restrict__ x, const u16* __restrict__ w, const u16* __restrict__ wr,
                                        char* __restrict__ ws, char* smem, int mblk, int nblk) {
  const int tid = threadIdx.x, l = tid & 63, wv = __builtin_amdgcn_readfirstlane(tid >> 6);
  const int wm = wv & 1, wn = wv >> 1, r32 = l & 31, h = l >> 5;
  const bool colw = (wm ^ (wn & 1) ^ (wn >> 1)) == 0;
  const int mx = wn >> 1;
  const unsigned sb = ldsbase(smem);
  const int m0 = mblk * 128, n0 = nblk * 128;
  unsigned aof[2], bof[2];
#pragma unroll
  for (int i = 0; i < 2; ++i) {
    int p = wv * 2 + i;
    int ra = p * 8 + (l >> 3), ca = (l & 7) ^ ((ra >> 1) & 7);
    aof[i] = (ra * D_ + ca * 8) * 2 + 1024 - i * 1024;
    int rb = p * 4 + (l >> 4), cb = (l & 15) ^ (((rb & 3) << 2) | ((rb >> 2) & 3));
    bof[i] = (rb * NQKV + cb * 8) * 2 + 1024 - i * 1024;
  }
  const char* abase = (const char*)(x + (size_t)m0 * D_) - 1024;
  const char* bbase = (const char*)(w + n0) - 1024;
  auto issue = [&](int c) {
    unsigned st = sb + (c & 3) * 32768 + wv * 2048;
    set_m0(st);
    glds_s<0>(aof[0], abase + c * 128); glds_s<1024>(aof[1], abase + c * 128);
    set_m0(st + 16384);
    glds_s<0>(bof[0], bbase + (size_t)c * 128 * NQKV); glds_s<1024>(bof[1], bbase + (size_t)c * 128 * NQKV);
  };
  if (RT && wv < 2) glds16(wr + min(wv * 512 + l * 8, D_ - 8), sb + 4 * 32768 + wv * 1024);
#pragma unroll
  for (int c = 0; c < 4; ++c) issue(c);
  unsigned aoff[4], boff[2];
  {
    int row = wm * 64 + mx * 32 + r32;
#pragma unroll
    for (int ks = 0; ks < 4; ++ks) aoff[ks] = row * 128 + (((ks * 2 + h) ^ ((row >> 1) & 7)) << 4);
    int g = l >> 4, q = (l >> 2) & 3, p = l & 3;
#pragma unroll
    for (int t = 0; t < 2; ++t) {
      int rr = 8 * (g >> 1) + 4 * t + q;
      int ch = (wn * 32 + 16 * (g & 1)) / 8 + (p >> 1);
      boff[t] = 16384 + rr * 256 + ((ch ^ (((rr & 3) << 2) | ((rr >> 2) & 3))) << 4) + 8 * (p & 1);
    }
  }
  const unsigned adelta = mx ? -4096 : 4096;
  f32x16 acc[2] = {};
  float s1 = 0, s2 = 0, rr = 0;
  struct Frag { u32x4 xa[2], wb, rv; };
  auto rd = [&](int c, int ks, Frag& f) {
    const unsigned st = sb + (c & 3) * 32768;
#pragma unroll
    for (int mt = 0; mt < 2; ++mt) f.xa[mt] = lds128(st + aoff[ks] + mt * adelta);
    u32x2 lo = tr_read(st + boff[0] + ks * 4096), hi = tr_read(st + boff[1] + ks * 4096);
    f.wb = u32x4{lo[0], lo[1], hi[0], hi[1]};
    if (RT) f.rv = lds128(sb + 4 * 32768 + c * 128 + ks * 32 + h * 16);
  };
  auto mm = [&](const Frag& f) {
#pragma unroll
    for (int mt = 0; mt < 2; ++mt) acc[mt] = ISV ? mfma32(f.xa[mt], f.wb, acc[mt]) : mfma32(f.wb, f.xa[mt], acc[mt]);
  };
  auto stat = [&](const Frag& f) {
    if (colw) {
#pragma unroll
      for (int j = 0; j < 4; ++j) s1 = dot2(f.wb[j], 0x3f803f80u, s1);
    } else {
#pragma unroll
      for (int j = 0; j < 4; ++j) {
        s1 = dot2(f.xa[0][j], 0x3f803f80u, s1); s2 = dot2(f.xa[0][j], f.xa[0][j], s2);
        if (RT) rr = dot2(f.xa[0][j], f.rv[j], rr);
      }
    }
  };
  Frag f0, f1, f2, f3;
  ST(0);
  WAIT_VM(12);
  bar();
  ST(1);
  rd(0, 0, f0); rd(0, 1, f1);
#pragma unroll
  for (int c = 0; c < 10; ++c) {
    rd(c, 2, f2); rd(c, 3, f3);
    mm(f0); mm(f1);
    stat(f0); stat(f1);
    if (c < 9) {
      if (c <= 6) WAIT_VM(8); else if (c == 7) WAIT_VM(4); else WAIT_VM(0);
      bar();
      if (c + 4 < 10) issue(c + 4);
    }
    if (c < 9) { rd(c + 1, 0, f0); rd(c + 1, 1, f1); }
    mm(f2); mm(f3);
    stat(f2); stat(f3);
  }
  ST(2);
  asm volatile("s_waitcnt lgkmcnt(0)" ::: "memory");
  LDSP(float) st = (LDSP(float))(size_t)sb;
  {
    const float a = s1 + __shfl_xor(s1, 32), b = s2 + __shfl_xor(s2, 32);
    if (colw) {
      if (h == 0) st[256 + wn * 32 + r32] = a;
    } else {
      const float mu = a * (1.f / D_);
      const float var = fmaxf(b * (1.f / D_) - mu * mu, 0.f);
      const float rs = 1.f / sqrtf(var + 1e-5f);
      const int tl = wm * 64 + mx * 32 + r32;
      if (RT) {
        float sw = 0;
#pragma unroll
        for (int i = 0; i < 5; ++i) sw = dot2(lds32(sb + 4 * 32768 + l * 20 + i * 4), 0x3f803f80u, sw);
#pragma unroll
        for (int o = 32; o; o >>= 1) sw += __shfl_xor(sw, o);
        rr += __shfl_xor(rr, 32);
        if (h == 0) {
          st[384 + tl] = (rr - mu * sw) * rs;
          ((float*)(ws + WS_MEAN))[m0 + tl] = mu; ((float*)(ws + WS_RSTD))[m0 + tl] = rs;
        }
      }
      if (h == 0) { st[tl] = mu; st[128 + tl] = rs; }
    }
  }
  bar();
  if (RT && wv == 0) {
    float a = st[384 + l], b = st[384 + l + 64]; int ia = l, ib = l + 64;
    if (b > a) { float t = a; a = b; b = t; int u = ia; ia = ib; ib = u; }
#pragma unroll
    for (int o = 32; o; o >>= 1) {
      float oa = __shfl_xor(a, o), ob = __shfl_xor(b, o); int oia = __shfl_xor(ia, o), oib = __shfl_xor(ib, o);
      bool w_ = oa > a || (oa == a && oia < ia);
      float na = w_ ? oa : a, nb1 = w_ ? a : oa, nb2 = w_ ? ob : b; int nia = w_ ? oia : ia, ni1 = w_ ? ia : oia, ni2 = w_ ? oib : ib;
      bool w2 = nb1 > nb2 || (nb1 == nb2 && ni1 < ni2);
      a = na; ia = nia; b = w2 ? nb1 : nb2; ib = w2 ? ni1 : ni2;
    }
    if (l == 0) {
      float* c = (float*)(ws + WS_CAND) + mblk * 4;
      c[0] = a; ((int*)c)[1] = m0 + ia; c[2] = b; ((int*)c)[3] = m0 + ib;
    }
  }
  if (!ISV) {
    const float sc = nblk < 5 ? QSCALE : 1.f;
#pragma unroll
    for (int mt = 0; mt < 2; ++mt) {
      const int tl = wm * 64 + (mt ^ mx) * 32;
      const float rs = st[128 + tl + r32] * sc, mu = st[tl + r32];
      char* tile = ws + WS_QK + (size_t)((m0 + tl) >> 5) * 81920 + r32 * 16;
#pragma unroll
      for (int gp = 0; gp < 2; ++gp) {
        unsigned pk[2][2];
#pragma unroll
        for (int e = 0; e < 2; ++e) {
          const int g = gp * 2 + e;
          f32x4 c4 = *(LDSP(f32x4))(st + 256 + wn * 32 + 8 * g + 4 * h);
          pk[e][0] = pack2((acc[mt][4 * g] - mu * c4[0]) * rs, (acc[mt][4 * g + 1] - mu * c4[1]) * rs);
          pk[e][1] = pack2((acc[mt][4 * g + 2] - mu * c4[2]) * rs, (acc[mt][4 * g + 3] - mu * c4[3]) * rs);
        }
        auto s0 = __builtin_amdgcn_permlane32_swap(pk[0][0], pk[1][0], false, false);
        auto s1_ = __builtin_amdgcn_permlane32_swap(pk[0][1], pk[1][1], false, false);
        const int grp = (n0 + wn * 32) / 8 + gp * 2 + h;
        *(u32x4*)(tile + grp * 512) = u32x4{s0[0], s1_[0], s0[1], s1_[1]};
      }
    }
  } else {
    const int nl = wn * 32 + r32;
    const int n = (n0 + nl) - 2 * D_, hd = n / DH, d = n - hd * DH;
    const float csn = st[256 + nl];
#pragma unroll
    for (int mt = 0; mt < 2; ++mt) {
      const int tl = wm * 64 + (mt ^ mx) * 32;
      char* img = ws + WS_V + (size_t)(hd * 64 + ((m0 + tl) >> 5)) * 1536;
#pragma unroll
      for (int s = 0; s < 2; ++s) {
        f32x4 ma = *(LDSP(f32x4))(st + tl + 16 * s + 4 * h), mb = *(LDSP(f32x4))(st + tl + 16 * s + 8 + 4 * h);
        f32x4 ra = *(LDSP(f32x4))(st + 128 + tl + 16 * s + 4 * h), rb = *(LDSP(f32x4))(st + 128 + tl + 16 * s + 8 + 4 * h);
        float v[8];
#pragma unroll
        for (int j = 0; j < 4; ++j) {
          v[j] = (acc[mt][8 * s + j] - ma[j] * csn) * ra[j];
          v[4 + j] = (acc[mt][8 * s + 4 + j] - mb[j] * csn) * rb[j];
        }
        char* dst = img + (s * 2 + h) * 384 + d * 16;
        *(u32x4*)dst = u32x4{pack2(v[0], v[1]), pack2(v[2], v[3]), pack2(v[4], v[5]), pack2(v[6], v[7])};
        if (d == DH - 1) *(u32x4*)(dst + 16) = u32x4{0x3f803f80u, 0x3f803f80u, 0x3f803f80u, 0x3f803f80u};
      }
    }
  }
  ST(3);
}

__global__ __launch_bounds__(512, 2) void k1_qkv(const u16* __restrict__ x, const u16* __restrict__ w,
                                                 const u16* __restrict__ wr, char* __restrict__ ws,
                                                 const u16* __restrict__ wg, const u16* __restrict__ wu,
                                                 const u16* __restrict__ wd, const u16* __restrict__ wo) {
  __shared__ __attribute__((aligned(16))) char smem[4 * 32768 + 2048];
  const int bid = blockIdx.x;
  if (bid >= 240) {
    const int g = (bid - 240) * 512 + threadIdx.x;
    const unsigned *a = (const unsigned*)wg, *b = (const unsigned*)wu, *c = (const unsigned*)wd, *d = (const unsigned*)wo;
    unsigned acc = 0;
#pragma unroll
    for (int j = 0; j < 5; ++j) {
      const int i = min(g + j * 8192, 34559) * 16;
      acc ^= a[i] ^ b[i] ^ c[i];
    }
#pragma unroll
    for (int j = 0; j < 2; ++j) acc ^= d[min(g + j * 8192, 12799) * 16];
    asm volatile("" :: "v"(acc));
    return;
  }
  const int t = (bid & 1) * 30 + (bid >> 3), mblk = ((bid & 7) >> 1) * 4 + (t & 3), nblk = t >> 2;
  if (nblk >= 10) k1_gemm<true, false>(x, w, wr, ws, smem, mblk, nblk);
  else if (nblk == 0) k1_gemm<false, true>(x, w, wr, ws, smem, mblk, nblk);
  else k1_gemm<false, false>(x, w, wr, ws, smem, mblk, nblk);
}

constexpr int K2_RING = 0, K2_WGU = 73728, K2_WD = K2_WGU + 40960, K2_XS = K2_WD + 20480, K2_SEL = K2_XS + 3072,
              K2_GP = K2_SEL + 256, K2_LDS = K2_GP + 1024;
constexpr unsigned PTHR = (127u + 100u) << 7;
constexpr float MBIAS = 60.f;

__device__ __forceinline__ unsigned pkmax3(unsigned a, unsigned b, unsigned c) {
  unsigned r; asm("v_pk_maximum3_f16 %0, %1, %2, %3" : "=v"(r) : "v"(a), "v"(b), "v"(c)); return r;
}
__device__ __forceinline__ void softmax_pack(const f32x16& s, u32x4& p0, u32x4& p1) {
#pragma unroll
  for (int i = 0; i < 4; ++i) {
    p0[i] = pack2(__builtin_amdgcn_exp2f(s[2 * i]), __builtin_amdgcn_exp2f(s[2 * i + 1]));
    p1[i] = pack2(__builtin_amdgcn_exp2f(s[8 + 2 * i]), __builtin_amdgcn_exp2f(s[9 + 2 * i]));
  }
}

__global__ __launch_bounds__(512, 2) void k2_attn(const u16* __restrict__ x, const u16* __restrict__ wg,
                                                  const u16* __restrict__ wu, const u16* __restrict__ wd,
                                                  const u16* __restrict__ wo, char* __restrict__ ws) {
  __shared__ __attribute__((aligned(16))) char smem[K2_LDS];
  const int tid = threadIdx.x, l = tid & 63, wv = __builtin_amdgcn_readfirstlane(tid >> 6);
  const int qg = wv & 3, hf = wv >> 2, r32 = l & 31, h = l >> 5;
  const int bid = blockIdx.x, head = (bid & 7) * 4 + ((bid >> 3) & 3), qb = bid >> 5;
  const bool moe = bid < NMOE;
  const unsigned sb = ldsbase(smem);
  const int odd = head & 1, g0 = 5 * (head >> 1) + 2 * odd;
  const unsigned c1off = odd * 512, c2off = (odd ? 8 : 1024);
  const char* ksrc = ws + WS_QK + (size_t)(hf * 32 + qg) * 81920 + (80 + g0) * 512;
  const char* vsrc = ws + WS_V + (size_t)(head * 64 + hf * 32 + qg) * 1536;
  const char* s0 = ksrc + l * 16;
  const char* s1 = h ? vsrc + r32 * 16 : ksrc + 1024 + r32 * 16;
  const char* s2 = vsrc + 512 + l * 16;
  const unsigned st1 = h ? 6144u : 327680u;
  auto issue = [&](int c) {
    unsigned dst = sb + K2_RING + (c % 3) * 24576 + hf * 12288 + qg * 3072;
    glds16(s0 + (size_t)c * 327680, dst);
    glds16(s1 + (size_t)c * st1, dst + 1024);
    glds16(s2 + (size_t)c * 6144, dst + 2048);
  };
  ST(0);
  u32x4 q1[2], q2[2];
#pragma unroll
  for (int t = 0; t < 2; ++t) {
    const char* qp = ws + WS_QK + (size_t)(qb * 8 + qg * 2 + t) * 81920 + g0 * 512;
    q1[t] = *(const u32x4*)(qp + c1off + l * 16);
    q2[t] = u32x4{*(const unsigned*)(qp + c2off + r32 * 16 + h * 4), 0u, 0u, 0u};
  }
  float ca = -INFINITY; int cia = 0x7fffffff;
  if (moe && wv == 0 && l < 32) { const float* cd = (const float*)(ws + WS_CAND); ca = cd[l * 2]; cia = ((const int*)cd)[l * 2 + 1]; }
  issue(0); issue(1);
  f32x16 o[2] = {};
  float m[2] = {0.f, 0.f};
  bool first = true;
  const unsigned koff = c1off + l * 16, k2off = c2off + r32 * 16 + h * 4, voff = 1536 + h * 384 + r32 * 16;

  f32x16 s[2][2];
  u32x4 vf[2][2];
  auto qk = [&](unsigned tile, f32x16* d) {
    u32x4 k1 = lds128(tile + koff);
    u32x4 k2 = {lds32(tile + k2off), 0x3f803f80u, 0u, 0u};
    const f32x16 z = {};
#pragma unroll
    for (int t = 0; t < 2; ++t) d[t] = mfma32(k2, q2[t], mfma32(k1, q1[t], z));
  };
  asm volatile("" :: "v"(q1[0]), "v"(q1[1]), "v"(q2[0][0]), "v"(q2[1][0]), "v"(ca), "v"(cia));
  WAIT_VM(3);
  bar();
  ST(1);
  if (moe) {
#pragma unroll
    for (int i = 0; i < 8; ++i) {
      int p = min(wv * 8 + i, 59);
      const u16* src;
      if (p < 40) src = ((l & 2) ? wu : wg) + (size_t)(p * 16 + (l >> 2)) * F_ + bid * 16 + (l & 1) * 8;
      else src = wd + (size_t)bid * 16 * D_ + (p - 40) * 512 + l * 8;
      glds16(src, sb + K2_WGU + p * 1024);
    }
  if (wv == 0) {
    float a = ca, b = -INFINITY; int ia = cia, ib = 0x7fffffff;
#pragma unroll
    for (int of = 32; of; of >>= 1) {
      float oa = __shfl_xor(a, of), ob = __shfl_xor(b, of); int oia = __shfl_xor(ia, of), oib = __shfl_xor(ib, of);
      bool w = oa > a || (oa == a && oia < ia);
      float na = w ? oa : a, nb1 = w ? a : oa, nb2 = w ? ob : b; int nia = w ? oia : ia, ni1 = w ? ia : oia, ni2 = w ? oib : ib;
      bool w2 = nb1 > nb2 || (nb1 == nb2 && ni1 < ni2);
      a = na; ia = nia; b = w2 ? nb1 : nb2; ib = w2 ? ni1 : ni2;
    }
#pragma unroll
    for (int i = 0; i < 3; ++i) {
      const int e = min(i * 64 + l, 159), tk = e >= 80;
      glds16(x + (size_t)(tk ? ib : ia) * D_ + (e - tk * 80) * 8, sb + K2_XS + i * 1024);
    }
    glds4(ws + ((l & 1) ? WS_RSTD : WS_MEAN) + ((l & 2) ? ib : ia) * 4, sb + K2_SEL);
    if (bid == 0 && l == 0) {
      int* g = (int*)(ws + WS_SEL);
      g[0] = ia; g[1] = ib;
      ((float*)g)[2] = 1.f / (1.f + __expf(-a)); ((float*)g)[3] = 1.f / (1.f + __expf(-b));
    }
  }
  }
  if (!moe) {
    const int p = (bid - NMOE) * 8 + wv;
    if (p < 800) glds16(wo + (size_t)p * 512 + l * 8, sb + K2_WGU + wv * 1024);
  }
  qk(sb + K2_RING + hf * 12288, s[0]);

#pragma unroll 1
  for (int c = 0; c < 8; ++c) {
    const unsigned st = sb + K2_RING + (c % 3) * 24576 + hf * 12288;
    const unsigned stn = sb + K2_RING + ((c + 1) % 3) * 24576 + hf * 12288;
#pragma unroll
    for (int T = 0; T < 4; ++T) {
      f32x16* cur = s[T & 1];
      f32x16* nxt = s[(T + 1) & 1];
      if (T == 3 && c < 7) {
        if (moe && c == 0) { if (wv == 0) WAIT_VM(12); else WAIT_VM(8); } else WAIT_VM(0);
        bar();
        ST(2 + c);
        if (c + 2 < 8) issue(c + 2);
        if (moe) {
      if (c == 1 && wv < 4) {
        const int g = l >> 4, q = (l >> 2) & 3, p = l & 3, col = l & 15;
        f32x4 ag = {0, 0, 0, 0}, au = {0, 0, 0, 0};
        const unsigned wa = sb + K2_WGU + wv * 10240 + (8 * g + q) * 64 + p * 8;
        const unsigned xa = sb + K2_XS + (col & 1) * 1280 + wv * 320 + g * 16;
        const unsigned fill = col == 2 ? 0x3f803f80u : 0u;
#pragma unroll
        for (int s = 0; s < 5; ++s) {
          u32x4 xb = lds128(xa + s * 64);
          if (col >= 2) xb = u32x4{fill, fill, fill, fill};
          u32x2 g0 = tr_read(wa + s * 2048), g1 = tr_read(wa + s * 2048 + 256);
          u32x2 u0 = tr_read(wa + s * 2048 + 32), u1 = tr_read(wa + s * 2048 + 256 + 32);
          ag = mfma16_32(u32x4{g0[0], g0[1], g1[0], g1[1]}, xb, ag);
          au = mfma16_32(u32x4{u0[0], u0[1], u1[0], u1[1]}, xb, au);
        }
        const float mu = __builtin_bit_cast(float, lds32(sb + K2_SEL + (col & 1) * 8));
        const float rs = __builtin_bit_cast(float, lds32(sb + K2_SEL + (col & 1) * 8 + 4));
#pragma unroll
        for (int i = 0; i < 4; ++i) {
          ag[i] = (ag[i] - mu * __shfl(ag[i], (l & 48) + 2)) * rs;
          au[i] = (au[i] - mu * __shfl(au[i], (l & 48) + 2)) * rs;
        }
        if (col < 2) {
          *(LDSP(f32x4))(size_t)(sb + K2_GP + wv * 256 + col * 64 + g * 16) = ag;
          *(LDSP(f32x4))(size_t)(sb + K2_GP + wv * 256 + 128 + col * 64 + g * 16) = au;
        }
      } else if (c == 2) {
        const int g = l >> 4, q = (l >> 2) & 3, p = l & 3, col = l & 15;
        f32x4 ag = {0, 0, 0, 0}, au = {0, 0, 0, 0};
#pragma unroll
        for (int w = 0; w < 4; ++w) {
          ag += *(LDSP(f32x4))(size_t)(sb + K2_GP + w * 256 + (col & 1) * 64 + g * 16);
          au += *(LDSP(f32x4))(size_t)(sb + K2_GP + w * 256 + 128 + (col & 1) * 64 + g * 16);
        }
        float hv[4];
#pragma unroll
        for (int i = 0; i < 4; ++i) hv[i] = ag[i] / (1.f + __expf(-ag[i])) * au[i];
        const u32x2 hb = {pack2(hv[0], hv[1]), pack2(hv[2], hv[3])};
        const unsigned da = sb + K2_WD + (4 * g + q) * 1280 + p * 8 + wv * 160;
        float* yp = (float*)(ws + WS_YP) + ((size_t)bid * 2 + (col & 1)) * D_ + 4 * g + wv * 80;
#pragma unroll
        for (int t = 0; t < 5; ++t) {
          f32x4 y = mfma16_16(tr_read(da + t * 32), hb, f32x4{0, 0, 0, 0});
          if (col < 2) *(f32x4*)(yp + t * 16) = y;
        }
      }
    }
      }
      vf[0][0] = lds128(st + T * KVT + voff);
      vf[0][1] = lds128(st + T * KVT + voff + 768);
      qk(T < 3 ? st + (T + 1) * KVT : stn, nxt);
      u32x4 p[2][2];
      softmax_pack(cur[0], p[0][0], p[0][1]);
      softmax_pack(cur[1], p[1][0], p[1][1]);
      unsigned pm = pkmax3(p[0][0][0], p[0][0][1], p[0][0][2]);
      pm = pkmax3(pm, p[0][0][3], p[0][1][0]); pm = pkmax3(pm, p[0][1][1], p[0][1][2]);
      unsigned pn = pkmax3(p[0][1][3], p[1][0][0], p[1][0][1]);
      pn = pkmax3(pn, p[1][0][2], p[1][0][3]); pn = pkmax3(pn, p[1][1][0], p[1][1][1]);
      pm = pkmax3(pm, pn, p[1][1][2]); pm = pkmax3(pm, p[1][1][3], p[1][1][3]);
      const bool over = ((pm + (0x7fffu - PTHR) * 0x10001u) & 0x80008000u) != 0u;
      if (__builtin_expect(first || __any(over), 0)) {
#pragma unroll
        for (int t = 0; t < 2; ++t) {
          float tm = fmaxf(cur[t][0], cur[t][1]);
#pragma unroll
          for (int i = 2; i < 16; ++i) tm = fmaxf(tm, cur[t][i]);
          tm = fmaxf(tm, __shfl_xor(tm, 32));
          tm += MBIAS;
          if (!first) tm = fmaxf(tm, 0.f);
          const float mn = m[t] + tm, dl = mn - m[t];
          const float al = first ? 0.f : __builtin_amdgcn_exp2f(-0.5f * dl);
          m[t] = mn;
#pragma unroll
          for (int i = 0; i < 12; ++i) o[t][i] = (o[t][i] * al) * al;
#pragma unroll
          for (int i = 0; i < 16; ++i) cur[t][i] -= dl;
#pragma unroll
          for (int i = 0; i < 16; ++i) nxt[t][i] -= dl;
          const unsigned uh = __builtin_bit_cast(unsigned, -mn) & 0xffff0000u;
          const float r1 = -mn - __builtin_bit_cast(float, uh);
          const unsigned um = __builtin_bit_cast(unsigned, r1) & 0xffff0000u;
          const float r2 = r1 - __builtin_bit_cast(float, um);
          q2[t][1] = h == 0 ? ((uh >> 16) | um) : (__builtin_bit_cast(unsigned, r2) >> 16);
          softmax_pack(cur[t], p[t][0], p[t][1]);
        }
        first = false;
      }
#pragma unroll
      for (int t = 0; t < 2; ++t) {
        o[t] = mfma32(vf[0][0], p[t][0], o[t]);
        o[t] = mfma32(vf[0][1], p[t][1], o[t]);
      }
    }
  }
  ST(9);
  bar();
  LDSP(float) mb = (LDSP(float))(size_t)(sb + qg * 6656);
  if (hf == 1) {
#pragma unroll
    for (int t = 0; t < 2; ++t) {
#pragma unroll
      for (int i = 0; i < 12; ++i) mb[(t * 13 + i) * 64 + l] = o[t][i];
      mb[(t * 13 + 12) * 64 + l] = m[t];
    }
  }
  bar();
  if (hf == 0) {
#pragma unroll
    for (int t = 0; t < 2; ++t) {
      const float mo = mb[(t * 13 + 12) * 64 + l];
      const float ea = m[t] + __builtin_amdgcn_logf(__shfl(o[t][8], r32 + 32));
      const float eb = mo + __builtin_amdgcn_logf(mb[(t * 13 + 8) * 64 + r32 + 32]);
      const float mm = fmaxf(ea, eb);
      const float a = __builtin_amdgcn_exp2f(0.5f * (m[t] - mm)), b = __builtin_amdgcn_exp2f(0.5f * (mo - mm));
      float r[12];
#pragma unroll
      for (int i = 0; i < 12; ++i) r[i] = (o[t][i] * a) * a + (mb[(t * 13 + i) * 64 + l] * b) * b;
      const float inv = 1.f / __shfl(r[8], r32 + 32);
      char* dst = ws + WS_O + ((size_t)(qb * 256 + qg * 64 + t * 32 + r32) * D_ + head * DH) * 2;
      *(u32x2*)(dst + h * 8) = u32x2{pack2(r[0] * inv, r[1] * inv), pack2(r[2] * inv, r[3] * inv)};
      *(u32x2*)(dst + 16 + h * 8) = u32x2{pack2(r[4] * inv, r[5] * inv), pack2(r[6] * inv, r[7] * inv)};
      if (h == 0) *(u32x2*)(dst + 32) = u32x2{pack2(r[8] * inv, r[9] * inv), pack2(r[10] * inv, r[11] * inv)};
    }
  }
  ST(10);
}

__global__ __launch_bounds__(256, 1) void k3_out(const u16* __restrict__ x, const u16* __restrict__ wo,
                                                 const char* __restrict__ ws, u16* __restrict__ out) {
  __shared__ __attribute__((aligned(16))) char smem[6 * 24576 + 2 * 6 * 640];
  const int tid = threadIdx.x, l = tid & 63, wv = __builtin_amdgcn_readfirstlane(tid >> 6);
  const int r32 = l & 31, h = l >> 5, bid = blockIdx.x;
  const int* sel = (const int*)(ws + WS_SEL);
  const int i0 = sel[0], i1 = sel[1];
  ST(0);
  const int mblk = (bid & 7) * 8 + ((bid >> 3) & 7), nblk = bid >> 6;
  const int m0 = mblk * 32, n0 = nblk * 160;
  const unsigned sb = ldsbase(smem);
  const u16* og = (const u16*)(ws + WS_O);
  unsigned sof[6];
#pragma unroll
  for (int i = 0; i < 6; ++i) {
    int p = wv * 6 + i;
    if (p < 4) { int ra = p * 8 + (l >> 3), ca = (l & 7) ^ ((ra >> 1) & 7); sof[i] = (ra * D_ + ca * 8) * 2; }
    else { int e = (p - 4) * 64 + l, rb = e / 20, cb = e - rb * 20; sof[i] = (rb * D_ + cb * 8) * 2; }
    sof[i] += 3072 - (i & 3) * 1024;
  }
  const char* abase = (const char*)(og + (size_t)m0 * D_) - 3072;
  const char* bbase = (const char*)(wo + n0) - 3072;
  auto issue = [&](int c) {
    const unsigned dst = sb + (c % 6) * 24576 + wv * 6144;
    const char* bb = bbase + (size_t)c * 128 * D_;
    const char* b0 = wv == 0 ? abase + c * 128 : bb;
    set_m0(dst);
    glds_s<0>(sof[0], b0); glds_s<1024>(sof[1], b0); glds_s<2048>(sof[2], b0); glds_s<3072>(sof[3], b0);
    set_m0(dst + 4096);
    glds_s<0>(sof[4], bb); glds_s<1024>(sof[5], bb);
  };
#pragma unroll 1
  for (int c = 0; c < 5; ++c) issue(c);
  const int er = tid >> 3, ec = (tid & 7) * 20, tok = m0 + er;
  const float mu = ((const float*)(ws + WS_MEAN))[tok], rs = ((const float*)(ws + WS_RSTD))[tok];
  u32x2 xv[5];
#pragma unroll
  for (int i = 0; i < 5; ++i) xv[i] = *(const u32x2*)(x + (size_t)tok * D_ + n0 + ec + i * 4);

  LDSP(float) mo = (LDSP(float))(size_t)(sb + 6 * 24576);
#pragma unroll
  for (int tk = 0; tk < 2; ++tk) {
    const unsigned rel = (unsigned)((tk ? i1 : i0) - m0);
    if (rel < 32u && tid < 240) {
      const int cg = tid % 40, pg = tid / 40;
      const float* yp = (const float*)(ws + WS_YP) + ((size_t)pg * 18 * 2 + tk) * D_ + n0 + cg * 4;
      f32x4 v[18];
#pragma unroll
      for (int w = 0; w < 18; ++w) v[w] = *(const f32x4*)(yp + (size_t)w * 2 * D_);
      f32x4 a = v[0];
#pragma unroll
      for (int w = 1; w < 18; ++w) a += v[w];
      *(LDSP(f32x4))(mo + (tk * 6 + pg) * 160 + cg * 4) = a;
    }
  }
  const unsigned aoff = r32 * 128 + (((wv * 2 + h) ^ ((r32 >> 1) & 7)) << 4);
  const int g = l >> 4, q = (l >> 2) & 3, p4 = l & 3;
  const unsigned boff = 4096 + (wv * 16 + 8 * (g >> 1) + q) * 320 + (16 * (g & 1) + 4 * p4) * 2;
  f32x16 acc[5] = {};
#pragma unroll 1
  for (int c = 0; c < 10; ++c) {
    if (c <= 5) WAIT_VM(24); else if (c == 6) WAIT_VM(18); else if (c == 7) WAIT_VM(12); else if (c == 8) WAIT_VM(6); else WAIT_VM(0);
    bar();
    if (c == 0) ST(1);
    if (c + 5 < 10) issue(c + 5);
    const unsigned st = sb + (c % 6) * 24576;
    u32x4 a = lds128(st + aoff);
#pragma unroll
    for (int nt = 0; nt < 5; ++nt) {
      u32x2 lo = tr_read(st + boff + nt * 64), hi = tr_read(st + boff + nt * 64 + 4 * 320);
      acc[nt] = mfma32(a, u32x4{lo[0], lo[1], hi[0], hi[1]}, acc[nt]);
    }
  }
  ST(2);
  bar();
  LDSP(float) pr = (LDSP(float))(size_t)(sb);
#pragma unroll
  for (int nt = 0; nt < 5; ++nt)
#pragma unroll
    for (int r = 0; r < 16; ++r) {
      int row = (r & 3) + 8 * (r >> 2) + 4 * h;
      pr[(wv * 32 + row) * 164 + nt * 32 + r32] = acc[nt][r];
    }
  bar();
  float upd[20];
#pragma unroll
  for (int i = 0; i < 20; ++i) upd[i] = 0.f;
  if (tok == i0 || tok == i1) {
    const int tk = tok == i0 ? 0 : 1;
    const float gate = ((const float*)sel)[2 + tk];
#pragma unroll
    for (int w = 0; w < 6; ++w) {
#pragma unroll
      for (int i = 0; i < 5; ++i) {
        f32x4 v = *(LDSP(f32x4))(mo + (tk * 6 + w) * 160 + ec + i * 4);
#pragma unroll
        for (int j = 0; j < 4; ++j) upd[i * 4 + j] += v[j];
      }
    }
#pragma unroll
    for (int i = 0; i < 20; ++i) upd[i] *= gate;
  }
#pragma unroll
  for (int i = 0; i < 5; ++i) {
    f32x4 v = *(LDSP(f32x4))(pr + er * 164 + ec + i * 4);
#pragma unroll
    for (int w = 1; w < 4; ++w) v += *(LDSP(f32x4))(pr + (w * 32 + er) * 164 + ec + i * 4);
    float xs[4] = {bflo(xv[i][0]), bfhi(xv[i][0]), bflo(xv[i][1]), bfhi(xv[i][1])};
    float ov[4];
#pragma unroll
    for (int j = 0; j < 4; ++j) ov[j] = xs[j] + (xs[j] - mu) * rs + v[j] + upd[i * 4 + j];
    *(u32x2*)(out + (size_t)tok * D_ + n0 + ec + i * 4) = u32x2{pack2(ov[0], ov[1]), pack2(ov[2], ov[3])};
  }
  ST(3);
}

extern "C" void kernel_launch(void* const* d_in, const int* in_sizes, int n_in, void* d_out, int out_size, void* d_ws,
                              size_t ws_size, hipStream_t stream) {
  const u16* x = (const u16*)d_in[0];
  const u16* wqkv = (const u16*)d_in[1];
  const u16* wo = (const u16*)d_in[2];
  const u16* wr = (const u16*)d_in[4];
  const u16* wg = (const u16*)d_in[5];
  const u16* wu = (const u16*)d_in[6];
  const u16* wd = (const u16*)d_in[7];
  char* ws = (char*)d_ws;
  k1_qkv<<<256, 512, 0, stream>>>(x, wqkv, wr, ws, wg, wu, wd, wo);
  k2_attn<<<256, 512, 0, stream>>>(x, wg, wu, wd, wo, ws);
  k3_out<<<256, 256, 0, stream>>>(x, wo, ws, (u16*)d_out);
}
```
